```python
import torch

def _softmax(x, axis=-1):
    return torch.softmax(x, dim=axis)
def _gelu(x):
    return torch.nn.functional.gelu(x, approximate="tanh")
def _silu(x):
    return torch.nn.functional.silu(x)
def _sigmoid(x):
    return torch.sigmoid(x)
def _layernorm(x, axis=-1, eps=1e-5):
    m = x.mean(dim=axis, keepdim=True)
    v = x.var(dim=axis, keepdim=True, unbiased=False)
    return (x - m) / torch.sqrt(v + eps)
def _scatter_add(tgt, idx, src, axis=0):
    return tgt.clone().index_add_(axis, idx, src)
def _segment_sum(src, seg, n):
    out = torch.zeros((n,) + src.shape[1:], dtype=src.dtype)
    return out.index_add_(0, seg, src)
def _pad(x, pw):
    flat = tuple(v for pair in reversed(pw) for v in pair)
    return torch.nn.functional.pad(x, flat)

kernel_name = 'recurrent_scan__sliding_block_cached__mixer_token_mlp__decode'

def setup_inputs():

    _w = (lambda a: (lambda t: t.to(torch.bfloat16) if t.is_floating_point() else t)(torch.as_tensor(a)))
    def _A_setup():
        B, Sq, D = 1, 1, 640
        Hq, Hkv, Sk, Dh = 32, 32, 32768, 192
        assert Hq % Hkv == 0, (Hq, Hkv)
        return {
            "x": _w((lambda *s: torch.randn(*s, dtype=torch.bfloat16) * 0.1)(B, Sq, D)),
            "k_cache": _w((lambda *s: torch.randn(*s, dtype=torch.bfloat16) * 0.8)(B, Hkv, Sk, Dh)),
            "v_cache": _w((lambda *s: torch.randn(*s, dtype=torch.bfloat16) * 3.2)(B, Hkv, Sk, Dh)),
            "wq": _w((lambda *s: torch.randn(*s, dtype=torch.bfloat16) * 0.8)(D, Hq * Dh) * (D ** -0.5)),
            "wo": _w((lambda *s: torch.randn(*s, dtype=torch.bfloat16) * 3.2)(Hq * Dh, D) * ((Hq * Dh) ** -0.5)),
        }
    _A_in = _A_setup()

    def _B_setup():
        B, S, D, F = 1, 1, 640, 1536
        return {
            "x": _w((lambda *s: torch.randn(*s, dtype=torch.bfloat16) * 0.1)(B, S, D)),
            "w1": _w((lambda *s: torch.randn(*s, dtype=torch.bfloat16) * 3.2)(S, F) * (S ** -0.5)),
            "b1": _w((lambda *s: torch.randn(*s, dtype=torch.bfloat16) * 3.2)(F) * 0.02),
            "w2": _w((lambda *s: torch.randn(*s, dtype=torch.bfloat16) * 3.2)(F, S) * (F ** -0.5)),
            "b2": _w((lambda *s: torch.randn(*s, dtype=torch.bfloat16) * 3.2)(S) * 0.02),
        }
    _B_in = _B_setup()

    return {
        "A_x": _A_in["x"],
        "A_k_cache": _A_in["k_cache"],
        "A_v_cache": _A_in["v_cache"],
        "A_wq": _A_in["wq"],
        "A_wo": _A_in["wo"],
        "B_x": _B_in["x"],
        "B_w1": _B_in["w1"],
        "B_b1": _B_in["b1"],
        "B_w2": _B_in["w2"],
        "B_b2": _B_in["b2"],
    }

def reference(A_x, A_k_cache, A_v_cache, A_wq, A_wo, B_x, B_w1, B_b1, B_w2, B_b2):
    def _A_ref(x, k_cache, v_cache, wq, wo):
        B, Sq, D = x.shape
        Hq, Dh = 32, 192
        Hkv, Sk = k_cache.shape[1], k_cache.shape[2]
        W = min(4096, max(256, Sk // 4))
        rep = Hq // Hkv
        q = (lambda x, *d: x.permute(*d))((x @ wq).reshape(B, Sq, Hq, Dh), 0, 2, 1, 3)
        k = (lambda x, n, axis: torch.repeat_interleave(x, n, dim=axis))(k_cache[:, :, Sk - W:, :], rep, axis=1)
        v = (lambda x, n, axis: torch.repeat_interleave(x, n, dim=axis))(v_cache[:, :, Sk - W:, :], rep, axis=1)
        scores = torch.einsum("bhqd,bhkd->bhqk", q, k) * (Dh ** -0.5)
        p = _softmax(scores, axis=-1)
        o = torch.einsum("bhqk,bhkd->bhqd", p, v)
        return (lambda x, *d: x.permute(*d))(o, 0, 2, 1, 3).reshape(B, Sq, Hq * Dh) @ wo

    def _B_ref(x, w1, b1, w2, b2):
        B, S, D = x.shape
        h = _layernorm(x, axis=-1)
        u = torch.einsum("bsd,sf->bdf", h, w1) + b1
        u = _gelu(u)
        y = torch.einsum("bdf,fs->bds", u, w2) + b2
        return (lambda x, *d: x.permute(*d))(y, 0, 2, 1)

    x = A_x
    xn = _layernorm(x, axis=-1)
    s = _A_ref(xn, A_k_cache, A_v_cache, A_wq, A_wo)
    g = _silu(_B_ref(xn, B_w1, B_b1, B_w2, B_b2))
    return x + s * g
```

```cpp
#include <hip/hip_runtime.h>
#include <hip/hip_bf16.h>
#include <stdint.h>

namespace {

typedef unsigned int u32;
typedef unsigned long long u64;
typedef u32 u32x3 __attribute__((ext_vector_type(3)));
typedef u32 u32x4 __attribute__((ext_vector_type(4)));
typedef __bf16 bf16x2 __attribute__((ext_vector_type(2)));
typedef __attribute__((address_space(1))) u32 gu32;
typedef __attribute__((address_space(1))) u64 gu64;
#define RLX_AGENT __ATOMIC_RELAXED, __HIP_MEMORY_SCOPE_AGENT

constexpr int DM = 640, HQ = 32, SK = 32768, DH = 192, WIN = 4096, FF = 1536;
constexpr int NSPLIT = 8, KPW = WIN / NSPLIT;
constexpr int NW = 16, NT = NW * 64;
constexpr int CB = DM / NSPLIT;
constexpr int FPH = FF / HQ;
constexpr float SCALE = 0.07216878364870322f;
constexpr float LOG2E = 1.4426950408889634f;

constexpr size_t WS_EPOCH = 0;
constexpr size_t WS_H1 = 1024;
constexpr int H1_REC = 256;
constexpr size_t WS_H2 = WS_H1 + (size_t)HQ * NSPLIT * H1_REC * 8;
constexpr u32 SPIN_CAP = 1u << 18;

__device__ __forceinline__ float bf2f(u32 h) { return __uint_as_float(h << 16); }
__device__ __forceinline__ float bflo(u32 w) { return __uint_as_float(w << 16); }
__device__ __forceinline__ float bfhi(u32 w) { return __uint_as_float(w & 0xffff0000u); }
__device__ __forceinline__ u32 f2bf(float x) {
    return (u32)__builtin_bit_cast(unsigned short, __float2bfloat16(x));
}
__device__ __forceinline__ float rbf(float x) { return bf2f(f2bf(x)); }
__device__ __forceinline__ float dot2(u32 a, u32 b, float c) {
    return __builtin_amdgcn_fdot2_f32_bf16(__builtin_bit_cast(bf16x2, a), __builtin_bit_cast(bf16x2, b), c, false);
}
template <int CTRL> __device__ __forceinline__ float dpp(float x) {
    return __builtin_bit_cast(float, __builtin_amdgcn_mov_dpp(__builtin_bit_cast(int, x), CTRL, 0xf, 0xf, true));
}
constexpr int XOR1 = 0xB1, XOR2 = 0x4E, XOR7 = 0x141, XOR8 = 0x128;
__device__ __forceinline__ float wave_sum(float x) {
    x += dpp<XOR1>(x); x += dpp<XOR2>(x); x += dpp<XOR7>(x); x += dpp<XOR8>(x);
    auto s = __builtin_amdgcn_permlane16_swap(__float_as_uint(x), __float_as_uint(x), false, false);
    x = __uint_as_float(s[0]) + __uint_as_float(s[1]);
    auto t = __builtin_amdgcn_permlane32_swap(__float_as_uint(x), __float_as_uint(x), false, false);
    return __uint_as_float(t[0]) + __uint_as_float(t[1]);
}
__device__ __forceinline__ float wave_max(float x) {
    x = fmaxf(x, dpp<XOR1>(x)); x = fmaxf(x, dpp<XOR2>(x)); x = fmaxf(x, dpp<XOR7>(x)); x = fmaxf(x, dpp<XOR8>(x));
    auto s = __builtin_amdgcn_permlane16_swap(__float_as_uint(x), __float_as_uint(x), false, false);
    x = fmaxf(__uint_as_float(s[0]), __uint_as_float(s[1]));
    auto t = __builtin_amdgcn_permlane32_swap(__float_as_uint(x), __float_as_uint(x), false, false);
    return fmaxf(__uint_as_float(t[0]), __uint_as_float(t[1]));
}
__device__ __forceinline__ float half_sum(float x) {
    auto t = __builtin_amdgcn_permlane32_swap(__float_as_uint(x), __float_as_uint(x), false, false);
    return __uint_as_float(t[0]) + __uint_as_float(t[1]);
}
__device__ __forceinline__ void st_gran(gu64* g, u32 tag, float v) {
    __hip_atomic_store(g, ((u64)tag << 32) | (u64)__float_as_uint(v), RLX_AGENT);
}
__device__ __forceinline__ void fma6(float (&acc)[6], float p, u32x3 w) {
    acc[0] = fmaf(p, bflo(w.x), acc[0]); acc[1] = fmaf(p, bfhi(w.x), acc[1]);
    acc[2] = fmaf(p, bflo(w.y), acc[2]); acc[3] = fmaf(p, bfhi(w.y), acc[3]);
    acc[4] = fmaf(p, bflo(w.z), acc[4]); acc[5] = fmaf(p, bfhi(w.z), acc[5]);
}
__device__ __forceinline__ void ln10(float (&v)[10]) {
    float s = 0.f;
#pragma unroll
    for (int k = 0; k < 10; ++k) s += v[k];
    const float mean = wave_sum(s) * (1.0f / DM);
    float ss = 0.f;
#pragma unroll
    for (int k = 0; k < 10; ++k) { const float d = v[k] - mean; ss = fmaf(d, d, ss); }
    const float var = wave_sum(ss) * (1.0f / DM);
    const float mb = rbf(mean), vb = rbf(var);
    const float den = rbf(sqrtf(rbf(vb + 1e-5f)));
    const float r = 1.0f / den;
#pragma unroll
    for (int k = 0; k < 10; ++k) {
        const float a = rbf(v[k] - mb);
        const float q = a * r;
        v[k] = rbf(fmaf(fmaf(-q, den, a), r, q));
    }
}
__device__ __forceinline__ float gelu_tanh(float x) {
    const float z = 0.7978845608028654f * (x + 0.044715f * x * x * x);
    return x / (1.0f + __builtin_amdgcn_exp2f(-2.0f * LOG2E * z));
}

constexpr int AUX_NT = 2;
constexpr int K0 = 4, K1 = 12, V0 = 6;

__global__ void __launch_bounds__(NT)
fused_decode(const u32* __restrict__ x, const u32* __restrict__ kc, const u32* __restrict__ vc,
             const u32* __restrict__ wq, const u32* __restrict__ wo,
             const unsigned short* __restrict__ w1, const unsigned short* __restrict__ b1,
             const unsigned short* __restrict__ w2, const unsigned short* __restrict__ b2,
             unsigned short* __restrict__ out, unsigned char* __restrict__ ws) {
    __shared__ __attribute__((aligned(16))) float lds[8192];
    float* xn_s = lds;
    float* hn_s = lds + 640;
    float* part = lds + 1280;
    float* pl = lds + 6400;
    float* ml = lds + 6912;
    u32* qb = (u32*)(lds + 6944);
    float* of = lds + 7040;
    float* yp = lds + 7232;
    float* red = lds + 7312;

    const int bid = blockIdx.x;
    const int h = (bid & 7) * 4 + ((bid >> 3) & 3), s = bid >> 5;
    const int tid = threadIdx.x, lane = tid & 63, hf = lane >> 5, c = lane & 31;
    const int wave = __builtin_amdgcn_readfirstlane(tid >> 6);

    const u32 tag = __hip_atomic_load((gu32*)(ws + WS_EPOCH), RLX_AGENT) + 1u;

    u32 xr[5];
#pragma unroll
    for (int k = 0; k < 5; ++k) xr[k] = x[lane * 5 + k];
    const int fidx = h * FPH + (lane < FPH ? lane : FPH - 1);
    const u32 w1r = w1[fidx], b1r = b1[fidx], w2r = w2[fidx];
    const u32 xe = x[(CB * s + (tid < CB ? tid : 0)) >> 1], b2r = b2[0];
    const auto rwq = __builtin_amdgcn_make_buffer_rsrc((void*)(wq + h * (DH / 2)), (short)0,
                                                       (int)(DM * HQ * DH * 2 - h * DH * 2), 0x00020000);
    const size_t kvoff = ((size_t)h * SK + (SK - WIN) + (size_t)s * KPW) * (DH / 2);
    const auto rk = __builtin_amdgcn_make_buffer_rsrc((void*)(kc + kvoff), (short)0, KPW * DH * 2, 0x00020000);
    const auto rv = __builtin_amdgcn_make_buffer_rsrc((void*)(vc + kvoff), (short)0, KPW * DH * 2, 0x00020000);

    u32x3 wqr[20];
    {
        const int vo = (40 * wave + hf) * (HQ * DH * 2) + 12 * c;
#pragma unroll
        for (int i = 0; i < 20; ++i) wqr[i] = __builtin_amdgcn_raw_buffer_load_b96(rwq, vo, i * 2 * (HQ * DH * 2), 0);
    }
    const int kvo = 12 * lane + wave * (32 * DH * 2);
    u32x3 kr[16], vr[16];
#pragma unroll
    for (int i = 0; i < K0; ++i) kr[i] = __builtin_amdgcn_raw_buffer_load_b96(rk, kvo, i * 768, AUX_NT);
    __builtin_amdgcn_sched_barrier(0);

    {
        float v[10];
#pragma unroll
        for (int k = 0; k < 5; ++k) { v[2 * k] = bflo(xr[k]); v[2 * k + 1] = bfhi(xr[k]); }
        ln10(v);
        if ((lane >> 2) == wave) {
#pragma unroll
            for (int k = 0; k < 10; ++k) xn_s[lane * 10 + k] = v[k];
        }
        if (wave == NW - 1) {
            ln10(v);
#pragma unroll
            for (int k = 0; k < 10; ++k) hn_s[lane * 10 + k] = v[k];
        }
    }
    __builtin_amdgcn_fence(__ATOMIC_RELEASE, "wavefront");
    __builtin_amdgcn_wave_barrier();
    __builtin_amdgcn_fence(__ATOMIC_ACQUIRE, "wavefront");

    float qa[6] = {0.f, 0.f, 0.f, 0.f, 0.f, 0.f};
    {
        const float* xs = xn_s + 40 * wave + hf;
#pragma unroll
        for (int i = 0; i < 10; ++i) fma6(qa, xs[2 * i], wqr[i]);
        __builtin_amdgcn_sched_barrier(0);
#pragma unroll
        for (int i = K0; i < K1; ++i) kr[i] = __builtin_amdgcn_raw_buffer_load_b96(rk, kvo, i * 768, AUX_NT);
        __builtin_amdgcn_sched_barrier(0);
#pragma unroll
        for (int i = 10; i < 20; ++i) fma6(qa, xs[2 * i], wqr[i]);
        __builtin_amdgcn_sched_barrier(0);
    }
#pragma unroll
    for (int i = K1; i < 16; ++i) kr[i] = __builtin_amdgcn_raw_buffer_load_b96(rk, kvo, i * 768, AUX_NT);
#pragma unroll
    for (int i = 0; i < V0; ++i) vr[i] = __builtin_amdgcn_raw_buffer_load_b96(rv, kvo, i * 768, AUX_NT);
    __builtin_amdgcn_sched_barrier(0);
#pragma unroll
    for (int k = 0; k < 6; ++k) qa[k] = half_sum(qa[k]);
    if (hf == 0) {
#pragma unroll
        for (int k = 0; k < 6; ++k) part[wave * DH + 6 * c + k] = qa[k];
    }
    __syncthreads();
    if (tid < DH / 2) {
        float a = 0.f, b = 0.f;
#pragma unroll
        for (int w = 0; w < NW; ++w) { a += part[w * DH + 2 * tid]; b += part[w * DH + 2 * tid + 1]; }
        qb[tid] = f2bf(a) | (f2bf(b) << 16);
    }
    __syncthreads();
    const u32 q0 = qb[3 * c], q1 = qb[3 * c + 1], q2 = qb[3 * c + 2];

    float xs_[16];
#pragma unroll
    for (int i = 0; i < 16; ++i) xs_[i] = dot2(kr[i].z, q2, dot2(kr[i].y, q1, dot2(kr[i].x, q0, 0.f)));
    asm volatile("" : "+v"(xs_[0]), "+v"(xs_[1]), "+v"(xs_[2]), "+v"(xs_[3]), "+v"(xs_[4]), "+v"(xs_[5]), "+v"(xs_[6]), "+v"(xs_[7]),
                      "+v"(xs_[8]), "+v"(xs_[9]), "+v"(xs_[10]), "+v"(xs_[11]), "+v"(xs_[12]), "+v"(xs_[13]), "+v"(xs_[14]), "+v"(xs_[15]) :: "memory");
    __builtin_amdgcn_sched_barrier(0);
#pragma unroll
    for (int i = V0; i < 16; ++i) vr[i] = __builtin_amdgcn_raw_buffer_load_b96(rv, kvo, i * 768, AUX_NT);
    u32x4 wor[3];
    const int cc = tid % 10, rg = tid / 10;
    {
        const auto rwo = __builtin_amdgcn_make_buffer_rsrc((void*)(wo + (size_t)h * DH * (DM / 2) + s * (CB / 2)), (short)0,
                                                           (int)(DH * DM * 2 - s * CB * 2), 0x00020000);
        const int vo = (tid < 640) ? (rg * 3 * DM * 2 + 16 * cc) : 0x7ffffff0;
#pragma unroll
        for (int j = 0; j < 3; ++j) wor[j] = __builtin_amdgcn_raw_buffer_load_b128(rwo, vo, j * DM * 2, 0);
    }
    __builtin_amdgcn_sched_barrier(0);
    {
        const float w1f = bf2f(w1r), b1f = bf2f(b1r), w2f = bf2f(w2r);
#pragma unroll
        for (int j = 0; j < 5; ++j) {
            const float hv = hn_s[CB * s + 5 * wave + j];
            const float u = rbf(rbf(hv * w1f) + b1f);
            float t = rbf(gelu_tanh(u)) * w2f;
            t = (lane < FPH) ? t : 0.f;
            t = wave_sum(t);
            if (lane == 0) yp[5 * wave + j] = t;
        }
    }
    float y[8];
#pragma unroll
    for (int j = 0; j < 8; ++j) {
        auto r = __builtin_amdgcn_permlane16_swap(__float_as_uint(xs_[j]), __float_as_uint(xs_[j + 8]), false, false);
        y[j] = __uint_as_float(r[0]) + __uint_as_float(r[1]);
    }
    float z[4];
#pragma unroll
    for (int j = 0; j < 4; ++j) { const float t1 = y[j] + dpp<XOR8>(y[j]), t2 = y[j + 4] + dpp<XOR8>(y[j + 4]); z[j] = (c & 8) ? t2 : t1; }
    float u2[2];
#pragma unroll
    for (int j = 0; j < 2; ++j) { const float t1 = z[j] + dpp<XOR7>(z[j]), t2 = z[j + 2] + dpp<XOR7>(z[j + 2]); u2[j] = (c & 4) ? t2 : t1; }
    float sc;
    { const float t1 = u2[0] + dpp<XOR2>(u2[0]), t2 = u2[1] + dpp<XOR2>(u2[1]); sc = (c & 2) ? t2 : t1; }
    sc += dpp<XOR1>(sc);
    sc = rbf(rbf(sc) * SCALE);
    const float mw = wave_max(sc);
    const float p = __builtin_amdgcn_exp2f((sc - mw) * LOG2E);
    const float lw = wave_sum((c & 1) ? 0.f : p);
    if ((c & 1) == 0) pl[(wave * 2 + hf) * 16 + (c >> 1)] = p;
    __builtin_amdgcn_fence(__ATOMIC_RELEASE, "wavefront");
    __builtin_amdgcn_wave_barrier();
    __builtin_amdgcn_fence(__ATOMIC_ACQUIRE, "wavefront");
    float pv[16];
    {
        const float4* pp = (const float4*)(pl + (wave * 2 + hf) * 16);
#pragma unroll
        for (int k = 0; k < 4; ++k) { const float4 t = pp[k]; pv[4 * k] = t.x; pv[4 * k + 1] = t.y; pv[4 * k + 2] = t.z; pv[4 * k + 3] = t.w; }
    }
    float oa[6] = {0.f, 0.f, 0.f, 0.f, 0.f, 0.f};
#pragma unroll
    for (int i = 0; i < 16; ++i) fma6(oa, pv[i], vr[i]);
#pragma unroll
    for (int k = 0; k < 6; ++k) oa[k] = half_sum(oa[k]);
    if (hf == 0) {
#pragma unroll
        for (int k = 0; k < 6; ++k) part[wave * DH + 6 * c + k] = oa[k];
    }
    if (lane == 0) { ml[2 * wave] = mw; ml[2 * wave + 1] = lw; }
    __syncthreads();

    gu64* h1 = (gu64*)(ws + WS_H1) + (size_t)h * NSPLIT * H1_REC;
    if (tid < DH) {
        float M = ml[0];
#pragma unroll
        for (int w = 1; w < NW; ++w) M = fmaxf(M, ml[2 * w]);
        float o = 0.f, L = 0.f;
#pragma unroll
        for (int w = 0; w < NW; ++w) {
            const float e = __builtin_amdgcn_exp2f((ml[2 * w] - M) * LOG2E);
            o = fmaf(e, part[w * DH + tid], o); L = fmaf(e, ml[2 * w + 1], L);
        }
        st_gran(h1 + s * H1_REC + tid, tag, o);
        if (tid == 0) { st_gran(h1 + s * H1_REC + DH, tag, M); st_gran(h1 + s * H1_REC + DH + 1, tag, L); }
        float go[NSPLIT], gm[NSPLIT], gl[NSPLIT];
        for (u32 spin = 0;; ++spin) {
            bool ok = true;
#pragma unroll
            for (int j = 0; j < NSPLIT; ++j) {
                const u64 a = __hip_atomic_load(h1 + j * H1_REC + tid, RLX_AGENT);
                const u64 b = __hip_atomic_load(h1 + j * H1_REC + DH, RLX_AGENT);
                const u64 d = __hip_atomic_load(h1 + j * H1_REC + DH + 1, RLX_AGENT);
                go[j] = __uint_as_float((u32)a); gm[j] = __uint_as_float((u32)b); gl[j] = __uint_as_float((u32)d);
                ok &= ((u32)(a >> 32) == tag) & ((u32)(b >> 32) == tag) & ((u32)(d >> 32) == tag);
            }
            if (__all(ok) || spin > SPIN_CAP) break;
            __builtin_amdgcn_s_sleep(1);
        }
        float MM = gm[0];
#pragma unroll
        for (int j = 1; j < NSPLIT; ++j) MM = fmaxf(MM, gm[j]);
        float oo = 0.f, LL = 0.f;
#pragma unroll
        for (int j = 0; j < NSPLIT; ++j) {
            const float e = __builtin_amdgcn_exp2f((gm[j] - MM) * LOG2E);
            oo = fmaf(e, go[j], oo); LL = fmaf(e, gl[j], LL);
        }
        of[tid] = rbf(oo / LL);
    }
    __syncthreads();

    if (tid < 640) {
        float a[8] = {0.f, 0.f, 0.f, 0.f, 0.f, 0.f, 0.f, 0.f};
#pragma unroll
        for (int j = 0; j < 3; ++j) {
            const float ov = of[3 * rg + j];
            a[0] = fmaf(ov, bflo(wor[j].x), a[0]); a[1] = fmaf(ov, bfhi(wor[j].x), a[1]);
            a[2] = fmaf(ov, bflo(wor[j].y), a[2]); a[3] = fmaf(ov, bfhi(wor[j].y), a[3]);
            a[4] = fmaf(ov, bflo(wor[j].z), a[4]); a[5] = fmaf(ov, bfhi(wor[j].z), a[5]);
            a[6] = fmaf(ov, bflo(wor[j].w), a[6]); a[7] = fmaf(ov, bfhi(wor[j].w), a[7]);
        }
        float4* dst = (float4*)(part + rg * CB + 8 * cc);
        dst[0] = make_float4(a[0], a[1], a[2], a[3]);
        dst[1] = make_float4(a[4], a[5], a[6], a[7]);
    }
    __syncthreads();
    if (tid < 640) {
        const int col = tid % CB, pt = tid / CB;
        float a = 0.f;
#pragma unroll
        for (int k = 0; k < 8; ++k) a += part[(pt * 8 + k) * CB + col];
        red[pt * CB + col] = a;
    }
    __syncthreads();
    gu64* h2 = (gu64*)(ws + WS_H2) + (size_t)s * HQ * CB * 2;
    if (tid < CB) {
        float a = 0.f;
#pragma unroll
        for (int k = 0; k < 8; ++k) a += red[k * CB + tid];
        st_gran(h2 + (h * CB + tid) * 2, tag, a);
        st_gran(h2 + (h * CB + tid) * 2 + 1, tag, yp[tid]);
    }
    if (h == 4 * s) {
        if (tid < 640) {
            const int col = tid % CB, hg = tid / CB;
            float sv, yv;
            for (u32 spin = 0;; ++spin) {
                bool ok = true; sv = 0.f; yv = 0.f;
#pragma unroll
                for (int j = 0; j < 4; ++j) {
                    const u64 a2 = __hip_atomic_load(h2 + ((hg * 4 + j) * CB + col) * 2, RLX_AGENT);
                    const u64 b2_ = __hip_atomic_load(h2 + ((hg * 4 + j) * CB + col) * 2 + 1, RLX_AGENT);
                    sv += __uint_as_float((u32)a2); yv += __uint_as_float((u32)b2_);
                    ok &= ((u32)(a2 >> 32) == tag) & ((u32)(b2_ >> 32) == tag);
                }
                if (__all(ok) || spin > SPIN_CAP) break;
                __builtin_amdgcn_s_sleep(1);
            }
            part[tid] = sv; part[640 + tid] = yv;
        }
        __syncthreads();
        if (tid < CB) {
            float sv = 0.f, yv = 0.f;
#pragma unroll
            for (int k = 0; k < 8; ++k) { sv += part[k * CB + tid]; yv += part[640 + k * CB + tid]; }
            const int d = CB * s + tid;
            const float yb = rbf(rbf(yv) + bf2f(b2r));
            const float g = rbf(yb / (1.0f + __expf(-yb)));
            const float sg = rbf(rbf(sv) * g);
            const float xv = (d & 1) ? bfhi(xe) : bflo(xe);
            out[d] = (unsigned short)f2bf(xv + sg);
            if (s == 0 && tid == 0) __hip_atomic_store((gu32*)(ws + WS_EPOCH), tag, RLX_AGENT);
        }
    }
}

}

extern "C" void kernel_launch(void* const* d_in, const int* in_sizes, int n_in,
                              void* d_out, int out_size, void* d_ws, size_t ws_size,
                              hipStream_t stream) {
    (void)in_sizes; (void)n_in; (void)out_size; (void)ws_size;
    hipLaunchKernelGGL(fused_decode, dim3(HQ * NSPLIT), dim3(NT), 0, stream,
                       (const u32*)d_in[0], (const u32*)d_in[1], (const u32*)d_in[2], (const u32*)d_in[3],
                       (const u32*)d_in[4], (const unsigned short*)d_in[6], (const unsigned short*)d_in[7],
                       (const unsigned short*)d_in[8], (const unsigned short*)d_in[9],
                       (unsigned short*)d_out, (unsigned char*)d_ws);
}
```

```cpp
#include <hip/hip_runtime.h>
#include <hip/hip_bf16.h>
#include <stdint.h>

namespace {

typedef unsigned int u32;
typedef unsigned long long u64;
typedef u32 u32x3 __attribute__((ext_vector_type(3)));
typedef u32 u32x4 __attribute__((ext_vector_type(4)));
typedef __bf16 bf16x2 __attribute__((ext_vector_type(2)));
typedef __attribute__((address_space(1))) u32 gu32;
typedef __attribute__((address_space(1))) u64 gu64;
#define RLX_AGENT __ATOMIC_RELAXED, __HIP_MEMORY_SCOPE_AGENT

constexpr int DM = 640, HQ = 32, SK = 32768, DH = 192, WIN = 4096, FF = 1536;
constexpr int NSPLIT = 8, KPW = WIN / NSPLIT;
constexpr int NW = 16, NT = NW * 64;
constexpr int CB = DM / NSPLIT;
constexpr int FPH = FF / HQ;
constexpr float SCALE = 0.07216878364870322f;
constexpr float LOG2E = 1.4426950408889634f;

constexpr size_t WS_EPOCH = 0;
constexpr size_t WS_H1 = 1024;
constexpr int H1_REC = 256;
constexpr size_t WS_H2 = WS_H1 + (size_t)HQ * NSPLIT * H1_REC * 8;
constexpr size_t WS_H0 = WS_H2 + (size_t)NSPLIT * HQ * CB * 2 * 8;
constexpr u32 SPIN_CAP = 1u << 18;

__device__ __forceinline__ float bf2f(u32 h) { return __uint_as_float(h << 16); }
__device__ __forceinline__ float bflo(u32 w) { return __uint_as_float(w << 16); }
__device__ __forceinline__ float bfhi(u32 w) { return __uint_as_float(w & 0xffff0000u); }
__device__ __forceinline__ u32 f2bf(float x) {
    return (u32)__builtin_bit_cast(unsigned short, __float2bfloat16(x));
}
__device__ __forceinline__ float rbf(float x) { return bf2f(f2bf(x)); }
__device__ __forceinline__ float dot2(u32 a, u32 b, float c) {
    return __builtin_amdgcn_fdot2_f32_bf16(__builtin_bit_cast(bf16x2, a), __builtin_bit_cast(bf16x2, b), c, false);
}
template <int CTRL> __device__ __forceinline__ float dpp(float x) {
    return __builtin_bit_cast(float, __builtin_amdgcn_mov_dpp(__builtin_bit_cast(int, x), CTRL, 0xf, 0xf, true));
}
constexpr int XOR1 = 0xB1, XOR2 = 0x4E, XOR7 = 0x141, XOR8 = 0x128;
__device__ __forceinline__ float wave_sum(float x) {
    x += dpp<XOR1>(x); x += dpp<XOR2>(x); x += dpp<XOR7>(x); x += dpp<XOR8>(x);
    auto s = __builtin_amdgcn_permlane16_swap(__float_as_uint(x), __float_as_uint(x), false, false);
    x = __uint_as_float(s[0]) + __uint_as_float(s[1]);
    auto t = __builtin_amdgcn_permlane32_swap(__float_as_uint(x), __float_as_uint(x), false, false);
    return __uint_as_float(t[0]) + __uint_as_float(t[1]);
}
__device__ __forceinline__ float wave_max(float x) {
    x = fmaxf(x, dpp<XOR1>(x)); x = fmaxf(x, dpp<XOR2>(x)); x = fmaxf(x, dpp<XOR7>(x)); x = fmaxf(x, dpp<XOR8>(x));
    auto s = __builtin_amdgcn_permlane16_swap(__float_as_uint(x), __float_as_uint(x), false, false);
    x = fmaxf(__uint_as_float(s[0]), __uint_as_float(s[1]));
    auto t = __builtin_amdgcn_permlane32_swap(__float_as_uint(x), __float_as_uint(x), false, false);
    return fmaxf(__uint_as_float(t[0]), __uint_as_float(t[1]));
}
__device__ __forceinline__ float half_sum(float x) {
    auto t = __builtin_amdgcn_permlane32_swap(__float_as_uint(x), __float_as_uint(x), false, false);
    return __uint_as_float(t[0]) + __uint_as_float(t[1]);
}
__device__ __forceinline__ void st_gran(gu64* g, u32 tag, float v) {
    __hip_atomic_store(g, ((u64)tag << 32) | (u64)__float_as_uint(v), RLX_AGENT);
}
__device__ __forceinline__ void fma6(float (&acc)[6], float p, u32x3 w) {
    acc[0] = fmaf(p, bflo(w.x), acc[0]); acc[1] = fmaf(p, bfhi(w.x), acc[1]);
    acc[2] = fmaf(p, bflo(w.y), acc[2]); acc[3] = fmaf(p, bfhi(w.y), acc[3]);
    acc[4] = fmaf(p, bflo(w.z), acc[4]); acc[5] = fmaf(p, bfhi(w.z), acc[5]);
}
__device__ __forceinline__ void ln10(float (&v)[10]) {
    float s = 0.f;
#pragma unroll
    for (int k = 0; k < 10; ++k) s += v[k];
    const float mean = wave_sum(s) * (1.0f / DM);
    float ss = 0.f;
#pragma unroll
    for (int k = 0; k < 10; ++k) { const float d = v[k] - mean; ss = fmaf(d, d, ss); }
    const float var = wave_sum(ss) * (1.0f / DM);
    const float mb = rbf(mean), vb = rbf(var);
    const float den = rbf(sqrtf(rbf(vb + 1e-5f)));
    const float r = 1.0f / den;
#pragma unroll
    for (int k = 0; k < 10; ++k) {
        const float a = rbf(v[k] - mb);
        const float q = a * r;
        v[k] = rbf(fmaf(fmaf(-q, den, a), r, q));
    }
}
__device__ __forceinline__ float gelu_tanh(float x) {
    const float z = 0.7978845608028654f * (x + 0.044715f * x * x * x);
    return x / (1.0f + __builtin_amdgcn_exp2f(-2.0f * LOG2E * z));
}

constexpr int AUX_NT = 2;
constexpr int K0 = 6, K1 = 10, V0 = 8;

__global__ void __launch_bounds__(NT)
fused_decode(const u32* __restrict__ x, const u32* __restrict__ kc, const u32* __restrict__ vc,
             const u32* __restrict__ wq, const u32* __restrict__ wo,
             const unsigned short* __restrict__ w1, const unsigned short* __restrict__ b1,
             const unsigned short* __restrict__ w2, const unsigned short* __restrict__ b2,
             unsigned short* __restrict__ out, unsigned char* __restrict__ ws) {
    __shared__ __attribute__((aligned(16))) float lds[8192];
    float* xn_s = lds;
    float* hn_s = lds + 640;
    float* part = lds + 1280;
    float* pl = lds + 6400;
    float* ml = lds + 6912;
    u32* qb = (u32*)(lds + 6944);
    float* of = lds + 7040;
    float* yp = lds + 7232;
    float* red = lds + 7312;

    const int bid = blockIdx.x;
    const int h = (bid & 7) * 4 + ((bid >> 3) & 3), s = bid >> 5;
    const int tid = threadIdx.x, lane = tid & 63, hf = lane >> 5, c = lane & 31;
    const int wave = __builtin_amdgcn_readfirstlane(tid >> 6);

    const u32 tag = __hip_atomic_load((gu32*)(ws + WS_EPOCH), RLX_AGENT) + 1u;

    u32 xr[5];
#pragma unroll
    for (int k = 0; k < 5; ++k) xr[k] = x[lane * 5 + k];
    const int fidx = h * FPH + (lane < FPH ? lane : FPH - 1);
    const u32 w1r = w1[fidx], b1r = b1[fidx], w2r = w2[fidx];
    const u32 xe = x[(CB * s + (tid < CB ? tid : 0)) >> 1], b2r = b2[0];
    const auto rwq = __builtin_amdgcn_make_buffer_rsrc((void*)(wq + h * (DH / 2)), (short)0,
                                                       (int)(DM * HQ * DH * 2 - h * DH * 2), 0x00020000);
    const size_t kvoff = ((size_t)h * SK + (SK - WIN) + (size_t)s * KPW) * (DH / 2);
    const auto rk = __builtin_amdgcn_make_buffer_rsrc((void*)(kc + kvoff), (short)0, KPW * DH * 2, 0x00020000);
    const auto rv = __builtin_amdgcn_make_buffer_rsrc((void*)(vc + kvoff), (short)0, KPW * DH * 2, 0x00020000);

    u32x3 wqr[3];
    {
        const int r0 = 80 * s + 5 * wave;
        wqr[0] = __builtin_amdgcn_raw_buffer_load_b96(rwq, (r0 + hf) * (HQ * DH * 2) + 12 * c, 0, AUX_NT);
        wqr[1] = __builtin_amdgcn_raw_buffer_load_b96(rwq, (r0 + hf) * (HQ * DH * 2) + 12 * c, 2 * (HQ * DH * 2), AUX_NT);
        wqr[2] = __builtin_amdgcn_raw_buffer_load_b96(rwq, r0 * (HQ * DH * 2) + 12 * c, 4 * (HQ * DH * 2), AUX_NT);
    }
    const int kvo = 12 * lane + wave * (32 * DH * 2);
    u32x3 kr[16], vr[16];
#pragma unroll
    for (int i = 0; i < K0; ++i) kr[i] = __builtin_amdgcn_raw_buffer_load_b96(rk, kvo, i * 768, AUX_NT);
    __builtin_amdgcn_sched_barrier(0);

    if (wave < 2) {
        float v[10];
#pragma unroll
        for (int k = 0; k < 5; ++k) { v[2 * k] = bflo(xr[k]); v[2 * k + 1] = bfhi(xr[k]); }
        ln10(v);
        if (wave == 0) {
#pragma unroll
            for (int k = 0; k < 10; ++k) xn_s[lane * 10 + k] = v[k];
        } else {
            ln10(v);
#pragma unroll
            for (int k = 0; k < 10; ++k) hn_s[lane * 10 + k] = v[k];
        }
    }
    __syncthreads();
    {
        float qa[6] = {0.f, 0.f, 0.f, 0.f, 0.f, 0.f};
        const float* xs = xn_s + 80 * s + 5 * wave;
        fma6(qa, xs[hf], wqr[0]);
        fma6(qa, xs[2 + hf], wqr[1]);
        fma6(qa, hf ? 0.f : xs[4], wqr[2]);
#pragma unroll
        for (int k = 0; k < 6; ++k) qa[k] = half_sum(qa[k]);
        if (hf == 0) {
#pragma unroll
            for (int k = 0; k < 6; ++k) part[wave * DH + 6 * c + k] = qa[k];
        }
    }
    __syncthreads();
    __builtin_amdgcn_sched_barrier(0);
    if (wave < 3) {
        gu64* h0 = (gu64*)(ws + WS_H0) + (size_t)h * NSPLIT * DH;
        float a = 0.f;
#pragma unroll
        for (int w = 0; w < NW; ++w) a += part[w * DH + tid];
        st_gran(h0 + s * DH + tid, tag, a);
#pragma unroll
        for (int i = K0; i < K1; ++i) kr[i] = __builtin_amdgcn_raw_buffer_load_b96(rk, kvo, i * 768, AUX_NT);
        float qs;
        for (u32 spin = 0;; ++spin) {
            bool ok = true; qs = 0.f;
#pragma unroll
            for (int j = 0; j < NSPLIT; ++j) {
                const u64 g = __hip_atomic_load(h0 + j * DH + tid, RLX_AGENT);
                qs += __uint_as_float((u32)g); ok &= ((u32)(g >> 32) == tag);
            }
            if (__all(ok) || spin > SPIN_CAP) break;
            __builtin_amdgcn_s_sleep(2);
        }
        ((unsigned short*)qb)[tid] = (unsigned short)f2bf(qs);
    } else {
#pragma unroll
        for (int i = K0; i < K1; ++i) kr[i] = __builtin_amdgcn_raw_buffer_load_b96(rk, kvo, i * 768, AUX_NT);
    }
    __builtin_amdgcn_sched_barrier(0);
#pragma unroll
    for (int i = K1; i < 16; ++i) kr[i] = __builtin_amdgcn_raw_buffer_load_b96(rk, kvo, i * 768, AUX_NT);
#pragma unroll
    for (int i = 0; i < V0; ++i) vr[i] = __builtin_amdgcn_raw_buffer_load_b96(rv, kvo, i * 768, AUX_NT);
    __builtin_amdgcn_sched_barrier(0);
    __syncthreads();
    const u32 q0 = qb[3 * c], q1 = qb[3 * c + 1], q2 = qb[3 * c + 2];

    float xs_[16];
#pragma unroll
    for (int i = 0; i < 16; ++i) xs_[i] = dot2(kr[i].z, q2, dot2(kr[i].y, q1, dot2(kr[i].x, q0, 0.f)));
    asm volatile("" : "+v"(xs_[0]), "+v"(xs_[1]), "+v"(xs_[2]), "+v"(xs_[3]), "+v"(xs_[4]), "+v"(xs_[5]), "+v"(xs_[6]), "+v"(xs_[7]),
                      "+v"(xs_[8]), "+v"(xs_[9]), "+v"(xs_[10]), "+v"(xs_[11]), "+v"(xs_[12]), "+v"(xs_[13]), "+v"(xs_[14]), "+v"(xs_[15]) :: "memory");
    __builtin_amdgcn_sched_barrier(0);
#pragma unroll
    for (int i = V0; i < 16; ++i) vr[i] = __builtin_amdgcn_raw_buffer_load_b96(rv, kvo, i * 768, AUX_NT);
    u32x4 wor[3];
    const int cc = tid % 10, rg = tid / 10;
    {
        const auto rwo = __builtin_amdgcn_make_buffer_rsrc((void*)(wo + (size_t)h * DH * (DM / 2) + s * (CB / 2)), (short)0,
                                                           (int)(DH * DM * 2 - s * CB * 2), 0x00020000);
        const int vo = (tid < 640) ? (rg * 3 * DM * 2 + 16 * cc) : 0x7ffffff0;
#pragma unroll
        for (int j = 0; j < 3; ++j) wor[j] = __builtin_amdgcn_raw_buffer_load_b128(rwo, vo, j * DM * 2, AUX_NT);
    }
    __builtin_amdgcn_sched_barrier(0);
    {
        const float w1f = bf2f(w1r), b1f = bf2f(b1r), w2f = bf2f(w2r);
#pragma unroll
        for (int j = 0; j < 5; ++j) {
            const float hv = hn_s[CB * s + 5 * wave + j];
            const float u = rbf(rbf(hv * w1f) + b1f);
            float t = rbf(gelu_tanh(u)) * w2f;
            t = (lane < FPH) ? t : 0.f;
            t = wave_sum(t);
            if (lane == 0) yp[5 * wave + j] = t;
        }
    }
    float y[8];
#pragma unroll
    for (int j = 0; j < 8; ++j) {
        auto r = __builtin_amdgcn_permlane16_swap(__float_as_uint(xs_[j]), __float_as_uint(xs_[j + 8]), false, false);
        y[j] = __uint_as_float(r[0]) + __uint_as_float(r[1]);
    }
    float z[4];
#pragma unroll
    for (int j = 0; j < 4; ++j) { const float t1 = y[j] + dpp<XOR8>(y[j]), t2 = y[j + 4] + dpp<XOR8>(y[j + 4]); z[j] = (c & 8) ? t2 : t1; }
    float u2[2];
#pragma unroll
    for (int j = 0; j < 2; ++j) { const float t1 = z[j] + dpp<XOR7>(z[j]), t2 = z[j + 2] + dpp<XOR7>(z[j + 2]); u2[j] = (c & 4) ? t2 : t1; }
    float sc;
    { const float t1 = u2[0] + dpp<XOR2>(u2[0]), t2 = u2[1] + dpp<XOR2>(u2[1]); sc = (c & 2) ? t2 : t1; }
    sc += dpp<XOR1>(sc);
    sc = rbf(rbf(sc) * SCALE);
    const float mw = wave_max(sc);
    const float p = __builtin_amdgcn_exp2f((sc - mw) * LOG2E);
    const float lw = wave_sum((c & 1) ? 0.f : p);
    if ((c & 1) == 0) pl[(wave * 2 + hf) * 16 + (c >> 1)] = p;
    __builtin_amdgcn_fence(__ATOMIC_RELEASE, "wavefront");
    __builtin_amdgcn_wave_barrier();
    __builtin_amdgcn_fence(__ATOMIC_ACQUIRE, "wavefront");
    float pv[16];
    {
        const float4* pp = (const float4*)(pl + (wave * 2 + hf) * 16);
#pragma unroll
        for (int k = 0; k < 4; ++k) { const float4 t = pp[k]; pv[4 * k] = t.x; pv[4 * k + 1] = t.y; pv[4 * k + 2] = t.z; pv[4 * k + 3] = t.w; }
    }
    float oa[6] = {0.f, 0.f, 0.f, 0.f, 0.f, 0.f};
#pragma unroll
    for (int i = 0; i < 16; ++i) fma6(oa, pv[i], vr[i]);
#pragma unroll
    for (int k = 0; k < 6; ++k) oa[k] = half_sum(oa[k]);
    if (hf == 0) {
#pragma unroll
        for (int k = 0; k < 6; ++k) part[wave * DH + 6 * c + k] = oa[k];
    }
    if (lane == 0) { ml[2 * wave] = mw; ml[2 * wave + 1] = lw; }
    __syncthreads();

    gu64* h1 = (gu64*)(ws + WS_H1) + (size_t)h * NSPLIT * H1_REC;
    if (tid < DH) {
        float M = ml[0];
#pragma unroll
        for (int w = 1; w < NW; ++w) M = fmaxf(M, ml[2 * w]);
        float o = 0.f, L = 0.f;
#pragma unroll
        for (int w = 0; w < NW; ++w) {
            const float e = __builtin_amdgcn_exp2f((ml[2 * w] - M) * LOG2E);
            o = fmaf(e, part[w * DH + tid], o); L = fmaf(e, ml[2 * w + 1], L);
        }
        st_gran(h1 + s * H1_REC + tid, tag, o);
        if (tid == 0) { st_gran(h1 + s * H1_REC + DH, tag, M); st_gran(h1 + s * H1_REC + DH + 1, tag, L); }
        float go[NSPLIT], gm[NSPLIT], gl[NSPLIT];
        for (u32 spin = 0;; ++spin) {
            bool ok = true;
#pragma unroll
            for (int j = 0; j < NSPLIT; ++j) {
                const u64 a = __hip_atomic_load(h1 + j * H1_REC + tid, RLX_AGENT);
                const u64 b = __hip_atomic_load(h1 + j * H1_REC + DH, RLX_AGENT);
                const u64 d = __hip_atomic_load(h1 + j * H1_REC + DH + 1, RLX_AGENT);
                go[j] = __uint_as_float((u32)a); gm[j] = __uint_as_float((u32)b); gl[j] = __uint_as_float((u32)d);
                ok &= ((u32)(a >> 32) == tag) & ((u32)(b >> 32) == tag) & ((u32)(d >> 32) == tag);
            }
            if (__all(ok) || spin > SPIN_CAP) break;
            __builtin_amdgcn_s_sleep(1);
        }
        float MM = gm[0];
#pragma unroll
        for (int j = 1; j < NSPLIT; ++j) MM = fmaxf(MM, gm[j]);
        float oo = 0.f, LL = 0.f;
#pragma unroll
        for (int j = 0; j < NSPLIT; ++j) {
            const float e = __builtin_amdgcn_exp2f((gm[j] - MM) * LOG2E);
            oo = fmaf(e, go[j], oo); LL = fmaf(e, gl[j], LL);
        }
        of[tid] = rbf(oo / LL);
    }
    __syncthreads();

    if (tid < 640) {
        float a[8] = {0.f, 0.f, 0.f, 0.f, 0.f, 0.f, 0.f, 0.f};
#pragma unroll
        for (int j = 0; j < 3; ++j) {
            const float ov = of[3 * rg + j];
            a[0] = fmaf(ov, bflo(wor[j].x), a[0]); a[1] = fmaf(ov, bfhi(wor[j].x), a[1]);
            a[2] = fmaf(ov, bflo(wor[j].y), a[2]); a[3] = fmaf(ov, bfhi(wor[j].y), a[3]);
            a[4] = fmaf(ov, bflo(wor[j].z), a[4]); a[5] = fmaf(ov, bfhi(wor[j].z), a[5]);
            a[6] = fmaf(ov, bflo(wor[j].w), a[6]); a[7] = fmaf(ov, bfhi(wor[j].w), a[7]);
        }
        float4* dst = (float4*)(part + rg * CB + 8 * cc);
        dst[0] = make_float4(a[0], a[1], a[2], a[3]);
        dst[1] = make_float4(a[4], a[5], a[6], a[7]);
    }
    __syncthreads();
    if (tid < 640) {
        const int col = tid % CB, pt = tid / CB;
        float a = 0.f;
#pragma unroll
        for (int k = 0; k < 8; ++k) a += part[(pt * 8 + k) * CB + col];
        red[pt * CB + col] = a;
    }
    __syncthreads();
    gu64* h2 = (gu64*)(ws + WS_H2) + (size_t)s * HQ * CB * 2;
    if (tid < CB) {
        float a = 0.f;
#pragma unroll
        for (int k = 0; k < 8; ++k) a += red[k * CB + tid];
        st_gran(h2 + (h * CB + tid) * 2, tag, a);
        st_gran(h2 + (h * CB + tid) * 2 + 1, tag, yp[tid]);
    }
    if (h == 4 * s) {
        if (tid < 640) {
            const int col = tid % CB, hg = tid / CB;
            float sv, yv;
            for (u32 spin = 0;; ++spin) {
                bool ok = true; sv = 0.f; yv = 0.f;
#pragma unroll
                for (int j = 0; j < 4; ++j) {
                    const u64 a2 = __hip_atomic_load(h2 + ((hg * 4 + j) * CB + col) * 2, RLX_AGENT);
                    const u64 b2_ = __hip_atomic_load(h2 + ((hg * 4 + j) * CB + col) * 2 + 1, RLX_AGENT);
                    sv += __uint_as_float((u32)a2); yv += __uint_as_float((u32)b2_);
                    ok &= ((u32)(a2 >> 32) == tag) & ((u32)(b2_ >> 32) == tag);
                }
                if (__all(ok) || spin > SPIN_CAP) break;
                __builtin_amdgcn_s_sleep(1);
            }
            part[tid] = sv; part[640 + tid] = yv;
        }
        __syncthreads();
        if (tid < CB) {
            float sv = 0.f, yv = 0.f;
#pragma unroll
            for (int k = 0; k < 8; ++k) { sv += part[k * CB + tid]; yv += part[640 + k * CB + tid]; }
            const int d = CB * s + tid;
            const float yb = rbf(rbf(yv) + bf2f(b2r));
            const float g = rbf(yb / (1.0f + __expf(-yb)));
            const float sg = rbf(rbf(sv) * g);
            const float xv = (d & 1) ? bfhi(xe) : bflo(xe);
            out[d] = (unsigned short)f2bf(xv + sg);
            if (s == 0 && tid == 0) __hip_atomic_store((gu32*)(ws + WS_EPOCH), tag, RLX_AGENT);
        }
    }
}

}

extern "C" void kernel_launch(void* const* d_in, const int* in_sizes, int n_in,
                              void* d_out, int out_size, void* d_ws, size_t ws_size,
                              hipStream_t stream) {
    (void)in_sizes; (void)n_in; (void)out_size; (void)ws_size;
    hipLaunchKernelGGL(fused_decode, dim3(HQ * NSPLIT), dim3(NT), 0, stream,
                       (const u32*)d_in[0], (const u32*)d_in[1], (const u32*)d_in[2], (const u32*)d_in[3],
                       (const u32*)d_in[4], (const unsigned short*)d_in[6], (const unsigned short*)d_in[7],
                       (const unsigned short*)d_in[8], (const unsigned short*)d_in[9],
                       (unsigned short*)d_out, (unsigned char*)d_ws);
}
```

```cpp
#include <hip/hip_runtime.h>
#include <hip/hip_bf16.h>
#include <stdint.h>

namespace {

typedef unsigned int u32;
typedef unsigned long long u64;
typedef u32 u32x3 __attribute__((ext_vector_type(3)));
typedef u32 u32x4 __attribute__((ext_vector_type(4)));
typedef __bf16 bf16x2 __attribute__((ext_vector_type(2)));
typedef __attribute__((address_space(1))) u32 gu32;
typedef __attribute__((address_space(1))) u64 gu64;
#define RLX_AGENT __ATOMIC_RELAXED, __HIP_MEMORY_SCOPE_AGENT

constexpr int DM = 640, HQ = 32, SK = 32768, DH = 192, WIN = 4096, FF = 1536;
constexpr int NSPLIT = 8, KPW = WIN / NSPLIT;
constexpr int NW = 16, NT = NW * 64;
constexpr int CB = DM / NSPLIT;
constexpr int FPH = FF / HQ;
constexpr float SCALE = 0.07216878364870322f;
constexpr float LOG2E = 1.4426950408889634f;

constexpr size_t WS_EPOCH = 0;
constexpr size_t WS_H1 = 1024;
constexpr int H1_REC = 256;
constexpr size_t WS_H2 = WS_H1 + (size_t)HQ * NSPLIT * H1_REC * 8;
constexpr size_t WS_H0 = WS_H2 + (size_t)NSPLIT * HQ * CB * 2 * 8;
constexpr u32 SPIN_CAP = 1u << 18;

__device__ __forceinline__ float bf2f(u32 h) { return __uint_as_float(h << 16); }
__device__ __forceinline__ float bflo(u32 w) { return __uint_as_float(w << 16); }
__device__ __forceinline__ float bfhi(u32 w) { return __uint_as_float(w & 0xffff0000u); }
__device__ __forceinline__ u32 f2bf(float x) {
    return (u32)__builtin_bit_cast(unsigned short, __float2bfloat16(x));
}
__device__ __forceinline__ float rbf(float x) { return bf2f(f2bf(x)); }
__device__ __forceinline__ float dot2(u32 a, u32 b, float c) {
    return __builtin_amdgcn_fdot2_f32_bf16(__builtin_bit_cast(bf16x2, a), __builtin_bit_cast(bf16x2, b), c, false);
}
template <int CTRL> __device__ __forceinline__ float dpp(float x) {
    return __builtin_bit_cast(float, __builtin_amdgcn_mov_dpp(__builtin_bit_cast(int, x), CTRL, 0xf, 0xf, true));
}
constexpr int XOR1 = 0xB1, XOR2 = 0x4E, XOR7 = 0x141, XOR8 = 0x128;
__device__ __forceinline__ float wave_sum(float x) {
    x += dpp<XOR1>(x); x += dpp<XOR2>(x); x += dpp<XOR7>(x); x += dpp<XOR8>(x);
    auto s = __builtin_amdgcn_permlane16_swap(__float_as_uint(x), __float_as_uint(x), false, false);
    x = __uint_as_float(s[0]) + __uint_as_float(s[1]);
    auto t = __builtin_amdgcn_permlane32_swap(__float_as_uint(x), __float_as_uint(x), false, false);
    return __uint_as_float(t[0]) + __uint_as_float(t[1]);
}
__device__ __forceinline__ float wave_max(float x) {
    x = fmaxf(x, dpp<XOR1>(x)); x = fmaxf(x, dpp<XOR2>(x)); x = fmaxf(x, dpp<XOR7>(x)); x = fmaxf(x, dpp<XOR8>(x));
    auto s = __builtin_amdgcn_permlane16_swap(__float_as_uint(x), __float_as_uint(x), false, false);
    x = fmaxf(__uint_as_float(s[0]), __uint_as_float(s[1]));
    auto t = __builtin_amdgcn_permlane32_swap(__float_as_uint(x), __float_as_uint(x), false, false);
    return fmaxf(__uint_as_float(t[0]), __uint_as_float(t[1]));
}
__device__ __forceinline__ float half_sum(float x) {
    auto t = __builtin_amdgcn_permlane32_swap(__float_as_uint(x), __float_as_uint(x), false, false);
    return __uint_as_float(t[0]) + __uint_as_float(t[1]);
}
__device__ __forceinline__ void st_gran(gu64* g, u32 tag, float v) {
    __hip_atomic_store(g, ((u64)tag << 32) | (u64)__float_as_uint(v), RLX_AGENT);
}
__device__ __forceinline__ void fma6(float (&acc)[6], float p, u32x3 w) {
    acc[0] = fmaf(p, bflo(w.x), acc[0]); acc[1] = fmaf(p, bfhi(w.x), acc[1]);
    acc[2] = fmaf(p, bflo(w.y), acc[2]); acc[3] = fmaf(p, bfhi(w.y), acc[3]);
    acc[4] = fmaf(p, bflo(w.z), acc[4]); acc[5] = fmaf(p, bfhi(w.z), acc[5]);
}
__device__ __forceinline__ void ln10(float (&v)[10]) {
    float s = 0.f;
#pragma unroll
    for (int k = 0; k < 10; ++k) s += v[k];
    const float mean = wave_sum(s) * (1.0f / DM);
    float ss = 0.f;
#pragma unroll
    for (int k = 0; k < 10; ++k) { const float d = v[k] - mean; ss = fmaf(d, d, ss); }
    const float var = wave_sum(ss) * (1.0f / DM);
    const float mb = rbf(mean), vb = rbf(var);
    const float den = rbf(sqrtf(rbf(vb + 1e-5f)));
    const float r = 1.0f / den;
#pragma unroll
    for (int k = 0; k < 10; ++k) {
        const float a = rbf(v[k] - mb);
        const float q = a * r;
        v[k] = rbf(fmaf(fmaf(-q, den, a), r, q));
    }
}
__device__ __forceinline__ float gelu_tanh(float x) {
    const float z = 0.7978845608028654f * (x + 0.044715f * x * x * x);
    return x / (1.0f + __builtin_amdgcn_exp2f(-2.0f * LOG2E * z));
}

constexpr int AUX_NT = 2;
constexpr int PS = 194, WPS = 84, LDS_FLOATS = 5328 + 64 * WPS;
constexpr int K0 = 6, K1 = 10, V0 = 8;

__global__ void __launch_bounds__(NT)
fused_decode(const u32* __restrict__ x, const u32* __restrict__ kc, const u32* __restrict__ vc,
             const u32* __restrict__ wq, const u32* __restrict__ wo,
             const unsigned short* __restrict__ w1, const unsigned short* __restrict__ b1,
             const unsigned short* __restrict__ w2, const unsigned short* __restrict__ b2,
             unsigned short* __restrict__ out, unsigned char* __restrict__ ws) {
    __shared__ __attribute__((aligned(16))) float lds[LDS_FLOATS];
    float* xn_s = lds;
    float* hn_s = lds + 640;
    float* part = lds + 1280;
    float* pl = lds + 4400;
    float* ml = lds + 4912;
    u32* qb = (u32*)(lds + 4944);
    float* fs = lds + 5040;
    u32* cnt = (u32*)(lds + 5048);
    float* yp = lds + 5056;
    float* of = lds + 5136;
    float* wp = lds + 5328;

    const int bid = blockIdx.x;
    const int h = (bid & 7) * 4 + ((bid >> 3) & 3), s = bid >> 5;
    const int tid = threadIdx.x, lane = tid & 63, hf = lane >> 5, c = lane & 31;
    const int wave = __builtin_amdgcn_readfirstlane(tid >> 6);
    const u32 tag = __hip_atomic_load((gu32*)(ws + WS_EPOCH), RLX_AGENT) + 1u;

    u32 xr[5];
#pragma unroll
    for (int k = 0; k < 5; ++k) xr[k] = x[lane * 5 + k];
    const int fidx = h * FPH + (lane < FPH ? lane : FPH - 1);
    const u32 w1r = w1[fidx], b1r = b1[fidx], w2r = w2[fidx];
    const u32 xe = x[(CB * s + (tid < 640 ? (tid >> 3) : 0)) >> 1], b2r = b2[0];
    const auto rwq = __builtin_amdgcn_make_buffer_rsrc((void*)(wq + h * (DH / 2)), (short)0,
                                                       (int)(DM * HQ * DH * 2 - h * DH * 2), 0x00020000);
    const size_t kvoff = ((size_t)h * SK + (SK - WIN) + (size_t)s * KPW) * (DH / 2);
    const auto rk = __builtin_amdgcn_make_buffer_rsrc((void*)(kc + kvoff), (short)0, KPW * DH * 2, 0x00020000);
    const auto rv = __builtin_amdgcn_make_buffer_rsrc((void*)(vc + kvoff), (short)0, KPW * DH * 2, 0x00020000);

    u32x3 wqr[3];
    {
        const int r0 = 80 * s + 5 * wave;
        wqr[0] = __builtin_amdgcn_raw_buffer_load_b96(rwq, (r0 + hf) * (HQ * DH * 2) + 12 * c, 0, AUX_NT);
        wqr[1] = __builtin_amdgcn_raw_buffer_load_b96(rwq, (r0 + hf) * (HQ * DH * 2) + 12 * c, 2 * (HQ * DH * 2), AUX_NT);
        wqr[2] = __builtin_amdgcn_raw_buffer_load_b96(rwq, r0 * (HQ * DH * 2) + 12 * c, 4 * (HQ * DH * 2), AUX_NT);
    }
    const int kvo = 12 * lane + wave * (32 * DH * 2);
    u32x3 kr[16], vr[16];
#pragma unroll
    for (int i = 0; i < K0; ++i) kr[i] = __builtin_amdgcn_raw_buffer_load_b96(rk, kvo, i * 768, AUX_NT);
    __builtin_amdgcn_sched_barrier(0);

    if (tid == 128) *cnt = 0u;
    if (wave < 2) {
        float v[10];
#pragma unroll
        for (int k = 0; k < 5; ++k) { v[2 * k] = bflo(xr[k]); v[2 * k + 1] = bfhi(xr[k]); }
        ln10(v);
        if (wave == 0) {
#pragma unroll
            for (int k = 0; k < 10; ++k) xn_s[lane * 10 + k] = v[k];
        } else {
            ln10(v);
#pragma unroll
            for (int k = 0; k < 10; ++k) hn_s[lane * 10 + k] = v[k];
        }
    }
    __syncthreads();
    {
        float qa[6] = {0.f, 0.f, 0.f, 0.f, 0.f, 0.f};
        const float* xs = xn_s + 80 * s + 5 * wave;
        fma6(qa, xs[hf], wqr[0]);
        fma6(qa, xs[2 + hf], wqr[1]);
        fma6(qa, hf ? 0.f : xs[4], wqr[2]);
#pragma unroll
        for (int k = 0; k < 6; ++k) qa[k] = half_sum(qa[k]);
        if (hf == 0) {
#pragma unroll
            for (int k = 0; k < 6; ++k) part[wave * PS + 6 * c + k] = qa[k];
        }
    }
    __syncthreads();
    __builtin_amdgcn_sched_barrier(0);
    if (wave < 3) {
        gu64* h0 = (gu64*)(ws + WS_H0) + (size_t)h * NSPLIT * DH;
        float a = 0.f;
#pragma unroll
        for (int w = 0; w < NW; ++w) a += part[w * PS + tid];
        st_gran(h0 + s * DH + tid, tag, a);
#pragma unroll
        for (int i = K0; i < K1; ++i) kr[i] = __builtin_amdgcn_raw_buffer_load_b96(rk, kvo, i * 768, AUX_NT);
        float qs;
        for (u32 spin = 0;; ++spin) {
            bool ok = true; qs = 0.f;
#pragma unroll
            for (int j = 0; j < NSPLIT; ++j) {
                const u64 g = __hip_atomic_load(h0 + j * DH + tid, RLX_AGENT);
                qs += __uint_as_float((u32)g); ok &= ((u32)(g >> 32) == tag);
            }
            if (__all(ok) || spin > SPIN_CAP) break;
            __builtin_amdgcn_s_sleep(2);
        }
        ((unsigned short*)qb)[tid] = (unsigned short)f2bf(qs);
    } else {
#pragma unroll
        for (int i = K0; i < K1; ++i) kr[i] = __builtin_amdgcn_raw_buffer_load_b96(rk, kvo, i * 768, AUX_NT);
    }
    __builtin_amdgcn_sched_barrier(0);
#pragma unroll
    for (int i = K1; i < 16; ++i) kr[i] = __builtin_amdgcn_raw_buffer_load_b96(rk, kvo, i * 768, AUX_NT);
#pragma unroll
    for (int i = 0; i < V0; ++i) vr[i] = __builtin_amdgcn_raw_buffer_load_b96(rv, kvo, i * 768, AUX_NT);
    __builtin_amdgcn_sched_barrier(0);
    __syncthreads();
    const u32 q0 = qb[3 * c], q1 = qb[3 * c + 1], q2 = qb[3 * c + 2];

    float xs_[16];
#pragma unroll
    for (int i = 0; i < 16; ++i) xs_[i] = dot2(kr[i].z, q2, dot2(kr[i].y, q1, dot2(kr[i].x, q0, 0.f)));
    asm volatile("" : "+v"(xs_[0]), "+v"(xs_[1]), "+v"(xs_[2]), "+v"(xs_[3]), "+v"(xs_[4]), "+v"(xs_[5]), "+v"(xs_[6]), "+v"(xs_[7]),
                      "+v"(xs_[8]), "+v"(xs_[9]), "+v"(xs_[10]), "+v"(xs_[11]), "+v"(xs_[12]), "+v"(xs_[13]), "+v"(xs_[14]), "+v"(xs_[15]) :: "memory");
    __builtin_amdgcn_sched_barrier(0);
    float y[8];
#pragma unroll
    for (int j = 0; j < 8; ++j) {
        auto r = __builtin_amdgcn_permlane16_swap(__float_as_uint(xs_[j]), __float_as_uint(xs_[j + 8]), false, false);
        y[j] = __uint_as_float(r[0]) + __uint_as_float(r[1]);
    }
    float z[4];
#pragma unroll
    for (int j = 0; j < 4; ++j) { const float t1 = y[j] + dpp<XOR8>(y[j]), t2 = y[j + 4] + dpp<XOR8>(y[j + 4]); z[j] = (c & 8) ? t2 : t1; }
    float u2[2];
#pragma unroll
    for (int j = 0; j < 2; ++j) { const float t1 = z[j] + dpp<XOR7>(z[j]), t2 = z[j + 2] + dpp<XOR7>(z[j + 2]); u2[j] = (c & 4) ? t2 : t1; }
    float sc;
    { const float t1 = u2[0] + dpp<XOR2>(u2[0]), t2 = u2[1] + dpp<XOR2>(u2[1]); sc = (c & 2) ? t2 : t1; }
    sc += dpp<XOR1>(sc);
    sc = rbf(rbf(sc) * SCALE);
    const float mw = wave_max(sc);
    const float p = __builtin_amdgcn_exp2f((sc - mw) * LOG2E);
    const float lw = wave_sum((c & 1) ? 0.f : p);
    if ((c & 1) == 0) pl[(wave * 2 + hf) * 16 + (c >> 1)] = p;
    gu64* h1 = (gu64*)(ws + WS_H1) + (size_t)h * NSPLIT * H1_REC;
    {
        u32 old = 0;
        if (lane == 0) {
            ml[wave] = mw; ml[16 + wave] = lw;
            old = __hip_atomic_fetch_add(cnt, 1u, __ATOMIC_ACQ_REL, __HIP_MEMORY_SCOPE_WORKGROUP);
        }
        old = __builtin_amdgcn_readfirstlane(old);
        __builtin_amdgcn_fence(__ATOMIC_ACQUIRE, "workgroup");
        if (old == NW - 1) {
            const float m_ = ml[lane & 15], l_ = ml[16 + (lane & 15)];
            float M = fmaxf(m_, dpp<XOR1>(m_)); M = fmaxf(M, dpp<XOR2>(M)); M = fmaxf(M, dpp<XOR7>(M)); M = fmaxf(M, dpp<XOR8>(M));
            float t = l_ * __builtin_amdgcn_exp2f((m_ - M) * LOG2E);
            t += dpp<XOR1>(t); t += dpp<XOR2>(t); t += dpp<XOR7>(t); t += dpp<XOR8>(t);
            if (lane < 2) st_gran(h1 + s * H1_REC + DH + lane, tag, lane ? t : M);
        }
    }
    __builtin_amdgcn_sched_barrier(0);
#pragma unroll
    for (int i = V0; i < 16; ++i) vr[i] = __builtin_amdgcn_raw_buffer_load_b96(rv, kvo, i * 768, AUX_NT);
    u32x4 wor[3];
    const int cc = tid % 10, rg = tid / 10;
    {
        const auto rwo = __builtin_amdgcn_make_buffer_rsrc((void*)(wo + (size_t)h * DH * (DM / 2) + s * (CB / 2)), (short)0,
                                                           (int)(DH * DM * 2 - s * CB * 2), 0x00020000);
        const int vo = (tid < 640) ? (rg * 3 * DM * 2 + 16 * cc) : 0x7ffffff0;
#pragma unroll
        for (int j = 0; j < 3; ++j) wor[j] = __builtin_amdgcn_raw_buffer_load_b128(rwo, vo, j * DM * 2, AUX_NT);
    }
    __builtin_amdgcn_sched_barrier(0);
    {
        const float w1f = bf2f(w1r), b1f = bf2f(b1r), w2f = bf2f(w2r);
#pragma unroll
        for (int j = 0; j < 5; ++j) {
            const float hv = hn_s[CB * s + 5 * wave + j];
            const float u = rbf(rbf(hv * w1f) + b1f);
            float t = rbf(gelu_tanh(u)) * w2f;
            t = (lane < FPH) ? t : 0.f;
            t = wave_sum(t);
            if (lane == 0) yp[5 * wave + j] = t;
        }
    }
    if (wave == NW - 1) {
        float gv;
        for (u32 spin = 0;; ++spin) {
            const u64 g = __hip_atomic_load(h1 + ((lane >> 1) & 7) * H1_REC + DH + (lane & 1), RLX_AGENT);
            gv = __uint_as_float((u32)g);
            if (__all((u32)(g >> 32) == tag) || spin > SPIN_CAP) break;
            __builtin_amdgcn_s_sleep(2);
        }
        const float other = dpp<XOR1>(gv);
        const float mj = (lane & 1) ? other : gv, lj = (lane & 1) ? gv : other;
        float MM = fmaxf(mj, dpp<XOR2>(mj)); MM = fmaxf(MM, dpp<XOR7>(MM)); MM = fmaxf(MM, dpp<XOR8>(MM));
        const float e = __builtin_amdgcn_exp2f((mj - MM) * LOG2E);
        float LL = e * lj; LL += dpp<XOR2>(LL); LL += dpp<XOR7>(LL); LL += dpp<XOR8>(LL);
        if (lane < 16 && (lane & 1) == 0) fs[lane >> 1] = e / LL;
    }
    __builtin_amdgcn_fence(__ATOMIC_RELEASE, "wavefront");
    __builtin_amdgcn_wave_barrier();
    __builtin_amdgcn_fence(__ATOMIC_ACQUIRE, "wavefront");
    float pv[16];
    {
        const float4* pp = (const float4*)(pl + (wave * 2 + hf) * 16);
#pragma unroll
        for (int k = 0; k < 4; ++k) { const float4 t = pp[k]; pv[4 * k] = t.x; pv[4 * k + 1] = t.y; pv[4 * k + 2] = t.z; pv[4 * k + 3] = t.w; }
    }
    float oa[6] = {0.f, 0.f, 0.f, 0.f, 0.f, 0.f};
#pragma unroll
    for (int i = 0; i < 16; ++i) fma6(oa, pv[i], vr[i]);
    {
        while (__hip_atomic_load(cnt, __ATOMIC_ACQUIRE, __HIP_MEMORY_SCOPE_WORKGROUP) < (u32)NW) __builtin_amdgcn_s_sleep(1);
        const float m_ = ml[lane & 15];
        float M = fmaxf(m_, dpp<XOR1>(m_)); M = fmaxf(M, dpp<XOR2>(M)); M = fmaxf(M, dpp<XOR7>(M)); M = fmaxf(M, dpp<XOR8>(M));
        const float e = __builtin_amdgcn_exp2f((mw - M) * LOG2E);
#pragma unroll
        for (int k = 0; k < 6; ++k) oa[k] = half_sum(oa[k]) * e;
    }
    if (hf == 0) {
#pragma unroll
        for (int k = 0; k < 6; ++k) part[wave * PS + 6 * c + k] = oa[k];
    }
    __syncthreads();
    if (tid < 4 * DH) {
        const int d = tid >> 2, qd = tid & 3;
        float a = 0.f;
#pragma unroll
        for (int k = 0; k < 4; ++k) a += part[(4 * qd + k) * PS + d];
        a += dpp<XOR1>(a); a += dpp<XOR2>(a);
        if (qd == 0) st_gran(h1 + s * H1_REC + d, tag, a);
    }
    if (tid < DH) {
        float f[NSPLIT];
#pragma unroll
        for (int j = 0; j < NSPLIT; ++j) f[j] = fs[j];
        float o;
        for (u32 spin = 0;; ++spin) {
            bool ok = true; o = 0.f;
#pragma unroll
            for (int j = 0; j < NSPLIT; ++j) {
                const u64 g = __hip_atomic_load(h1 + j * H1_REC + tid, RLX_AGENT);
                o = fmaf(f[j], __uint_as_float((u32)g), o);
                ok &= ((u32)(g >> 32) == tag);
            }
            if (__all(ok) || spin > SPIN_CAP) break;
            __builtin_amdgcn_s_sleep(1);
        }
        of[tid] = rbf(o);
    }
    __syncthreads();
    if (tid < 640) {
        float a[8] = {0.f, 0.f, 0.f, 0.f, 0.f, 0.f, 0.f, 0.f};
#pragma unroll
        for (int j = 0; j < 3; ++j) {
            const float o = of[3 * rg + j];
            a[0] = fmaf(o, bflo(wor[j].x), a[0]); a[1] = fmaf(o, bfhi(wor[j].x), a[1]);
            a[2] = fmaf(o, bflo(wor[j].y), a[2]); a[3] = fmaf(o, bfhi(wor[j].y), a[3]);
            a[4] = fmaf(o, bflo(wor[j].z), a[4]); a[5] = fmaf(o, bfhi(wor[j].z), a[5]);
            a[6] = fmaf(o, bflo(wor[j].w), a[6]); a[7] = fmaf(o, bfhi(wor[j].w), a[7]);
        }
        float4* dst = (float4*)(wp + rg * WPS + 8 * cc);
        dst[0] = make_float4(a[0], a[1], a[2], a[3]);
        dst[1] = make_float4(a[4], a[5], a[6], a[7]);
    }
    __syncthreads();
    gu64* h2 = (gu64*)(ws + WS_H2) + (size_t)s * HQ * CB * 2;
    if (tid < 640) {
        const int col = tid >> 3, pt = tid & 7;
        float a = 0.f;
#pragma unroll
        for (int k = 0; k < 8; ++k) a += wp[(pt + 8 * k) * WPS + col];
        a += dpp<XOR1>(a); a += dpp<XOR2>(a); a += dpp<XOR7>(a);
        if (pt < 2) st_gran(h2 + (h * CB + col) * 2 + pt, tag, pt ? yp[col] : a);
    }
    if (h == 4 * s) {
        if (tid < 640) {
            const int col = tid % CB, hg = tid / CB;
            float sv, yv;
            for (u32 spin = 0;; ++spin) {
                bool ok = true; sv = 0.f; yv = 0.f;
#pragma unroll
                for (int j = 0; j < 4; ++j) {
                    const u64 a2 = __hip_atomic_load(h2 + ((hg * 4 + j) * CB + col) * 2, RLX_AGENT);
                    const u64 b2_ = __hip_atomic_load(h2 + ((hg * 4 + j) * CB + col) * 2 + 1, RLX_AGENT);
                    sv += __uint_as_float((u32)a2); yv += __uint_as_float((u32)b2_);
                    ok &= ((u32)(a2 >> 32) == tag) & ((u32)(b2_ >> 32) == tag);
                }
                if (__all(ok) || spin > SPIN_CAP) break;
                __builtin_amdgcn_s_sleep(1);
            }
            part[tid] = sv; part[640 + tid] = yv;
        }
        __syncthreads();
        if (tid < 640 && (tid & 7) == 0) {
            const int col = tid >> 3;
            float sv = 0.f, yv = 0.f;
#pragma unroll
            for (int k = 0; k < 8; ++k) { sv += part[k * CB + col]; yv += part[640 + k * CB + col]; }
            const int d = CB * s + col;
            const float yb = rbf(rbf(yv) + bf2f(b2r));
            const float g = rbf(yb / (1.0f + __expf(-yb)));
            const float sg = rbf(rbf(sv) * g);
            const float xv = (d & 1) ? bfhi(xe) : bflo(xe);
            out[d] = (unsigned short)f2bf(xv + sg);
            if (s == 0 && tid == 0) __hip_atomic_store((gu32*)(ws + WS_EPOCH), tag, RLX_AGENT);
        }
    }
}

}

extern "C" void kernel_launch(void* const* d_in, const int* in_sizes, int n_in,
                              void* d_out, int out_size, void* d_ws, size_t ws_size,
                              hipStream_t stream) {
    (void)in_sizes; (void)n_in; (void)out_size; (void)ws_size;
    hipLaunchKernelGGL(fused_decode, dim3(HQ * NSPLIT), dim3(NT), 0, stream,
                       (const u32*)d_in[0], (const u32*)d_in[1], (const u32*)d_in[2], (const u32*)d_in[3],
                       (const u32*)d_in[4], (const unsigned short*)d_in[6], (const unsigned short*)d_in[7],
                       (const unsigned short*)d_in[8], (const unsigned short*)d_in[9],
                       (unsigned short*)d_out, (unsigned char*)d_ws);
}
```
